```python
import torch

def _softmax(x, axis=-1):
    return torch.softmax(x, dim=axis)
def _gelu(x):
    return torch.nn.functional.gelu(x, approximate="tanh")
def _silu(x):
    return torch.nn.functional.silu(x)
def _sigmoid(x):
    return torch.sigmoid(x)
def _layernorm(x, axis=-1, eps=1e-5):
    m = x.mean(dim=axis, keepdim=True)
    v = x.var(dim=axis, keepdim=True, unbiased=False)
    return (x - m) / torch.sqrt(v + eps)
def _scatter_add(tgt, idx, src, axis=0):
    return tgt.clone().index_add_(axis, idx, src)
def _segment_sum(src, seg, n):
    out = torch.zeros((n,) + src.shape[1:], dtype=src.dtype)
    return out.index_add_(0, seg, src)
def _pad(x, pw):
    flat = tuple(v for pair in reversed(pw) for v in pair)
    return torch.nn.functional.pad(x, flat)

kernel_name = 'gated_mix__sliding_block_cached__dense_activation_silu__decode'

def setup_inputs():

    _w = (lambda a: (lambda t: t.to(torch.bfloat16) if t.is_floating_point() else t)(torch.as_tensor(a)))
    def _A_setup():
        B, Sq, D = 8, 1, 448
        Hq, Hkv, Sk, Dh = 16, 8, 4096, 256
        assert Hq % Hkv == 0, (Hq, Hkv)
        return {
            "x": _w((lambda *s: torch.randn(*s, dtype=torch.bfloat16) * 0.2)(B, Sq, D)),
            "k_cache": _w((lambda *s: torch.randn(*s, dtype=torch.bfloat16) * 3.2)(B, Hkv, Sk, Dh)),
            "v_cache": _w((lambda *s: torch.randn(*s, dtype=torch.bfloat16) * 3.2)(B, Hkv, Sk, Dh)),
            "wq": _w((lambda *s: torch.randn(*s, dtype=torch.bfloat16) * 3.2)(D, Hq * Dh) * (D ** -0.5)),
            "wo": _w((lambda *s: torch.randn(*s, dtype=torch.bfloat16) * 3.2)(Hq * Dh, D) * ((Hq * Dh) ** -0.5)),
        }
    _A_in = _A_setup()

    def _B_setup():
        B, S, D, F = 8, 1, 448, 1792
        return {
            "x": _w((lambda *s: torch.randn(*s, dtype=torch.bfloat16) * 0.2)(B, S, D)),
            "w1": _w((lambda *s: torch.randn(*s, dtype=torch.bfloat16) * 3.2)(D, F) * (D ** -0.5)),
            "w2": _w((lambda *s: torch.randn(*s, dtype=torch.bfloat16) * 3.2)(F, D) * (F ** -0.5)),
        }
    _B_in = _B_setup()

    return {
        "A_x": _A_in["x"],
        "A_k_cache": _A_in["k_cache"],
        "A_v_cache": _A_in["v_cache"],
        "A_wq": _A_in["wq"],
        "A_wo": _A_in["wo"],
        "B_x": _B_in["x"],
        "B_w1": _B_in["w1"],
        "B_w2": _B_in["w2"],
        "gate_w": _w((lambda *s: torch.randn(*s, dtype=torch.bfloat16) * 0.2)(448) * 16.0),
    }

def reference(A_x, A_k_cache, A_v_cache, A_wq, A_wo, B_x, B_w1, B_w2, gate_w):
    def _A_ref(x, k_cache, v_cache, wq, wo):
        B, Sq, D = x.shape
        Hq, Dh = 16, 256
        Hkv, Sk = k_cache.shape[1], k_cache.shape[2]
        W = min(4096, max(256, Sk // 4))
        rep = Hq // Hkv
        q = (lambda x, *d: x.permute(*d))((x @ wq).reshape(B, Sq, Hq, Dh), 0, 2, 1, 3)
        k = (lambda x, n, axis: torch.repeat_interleave(x, n, dim=axis))(k_cache[:, :, Sk - W:, :], rep, axis=1)
        v = (lambda x, n, axis: torch.repeat_interleave(x, n, dim=axis))(v_cache[:, :, Sk - W:, :], rep, axis=1)
        scores = torch.einsum("bhqd,bhkd->bhqk", q, k) * (Dh ** -0.5)
        p = _softmax(scores, axis=-1)
        o = torch.einsum("bhqk,bhkd->bhqd", p, v)
        return (lambda x, *d: x.permute(*d))(o, 0, 2, 1, 3).reshape(B, Sq, Hq * Dh) @ wo

    def _B_ref(x, w1, w2):
        h = torch.einsum("bsd,df->bsf", x, w1)
        h = _silu(h)
        out = torch.einsum("bsf,fd->bsd", h, w2)
        return out

    x = A_x
    g = _sigmoid((x * gate_w).sum(-1, keepdims=True))
    a = _A_ref(x, A_k_cache, A_v_cache, A_wq, A_wo)
    b = _B_ref(x, B_w1, B_w2)
    return g * a + (1 - g) * b
```

```cpp
#include <hip/hip_runtime.h>
#include <hip/hip_bf16.h>
#include <stdint.h>

typedef unsigned short u16;
typedef unsigned u32x4 __attribute__((ext_vector_type(4)));
typedef float f32x4 __attribute__((ext_vector_type(4)));
typedef __bf16 bf16x2 __attribute__((ext_vector_type(2)));

#define MIMIC 0

constexpr int B = 8, D = 448, HQ = 16, HKV = 8, SK = 4096, DH = 256, W = 1024, F = 1792;

__device__ __forceinline__ float bf2f(u16 v) { return __builtin_bit_cast(float, (unsigned)v << 16); }
__device__ __forceinline__ float rbf(float f) { return (float)(__bf16)f; }
__device__ __forceinline__ u16 f2bf(float f) { return __builtin_bit_cast(u16, (__bf16)f); }
__device__ __forceinline__ float lo(unsigned v) { return __builtin_bit_cast(float, v << 16); }
__device__ __forceinline__ float hi(unsigned v) { return __builtin_bit_cast(float, v & 0xffff0000u); }

__global__ void __launch_bounds__(256) k1(const u16* x, const u16* wq, const u16* w1, float* q, float* h) {
  __shared__ float xs[B][D];
  __shared__ float red[64][4][8][8];
  const int tid = threadIdx.x;
  for (int i = tid; i < B * D; i += 256) xs[i / D][i % D] = bf2f(x[i]);
  __syncthreads();
  const bool isq = blockIdx.x < 128;
  const u16* w = isq ? wq : w1;
  const int ld = isq ? 4096 : F;
  const int c0 = (isq ? blockIdx.x : blockIdx.x - 128) * 32;
  const int ch = tid & 3, rg = tid >> 2;
  float acc[8][8];
  for (int b = 0; b < 8; ++b) for (int c = 0; c < 8; ++c) acc[b][c] = 0.f;
  for (int r = rg; r < D; r += 64) {
    u32x4 v = *(const u32x4*)(w + (size_t)r * ld + c0 + ch * 8);
    float wf[8] = {lo(v.x), hi(v.x), lo(v.y), hi(v.y), lo(v.z), hi(v.z), lo(v.w), hi(v.w)};
#pragma unroll
    for (int b = 0; b < 8; ++b) { float xv = xs[b][r];
#pragma unroll
      for (int c = 0; c < 8; ++c) acc[b][c] += xv * wf[c]; }
  }
  for (int b = 0; b < 8; ++b) for (int c = 0; c < 8; ++c) red[rg][ch][b][c] = acc[b][c];
  __syncthreads();
  {
    const int b = tid >> 5, c = tid & 31;
    float s = 0.f;
    for (int g = 0; g < 64; ++g) s += red[g][c >> 3][b][c & 7];
    s = rbf(s);
    if (isq) q[b * 4096 + c0 + c] = s;
    else { float sv = s / (1.f + __expf(-s)); h[b * F + c0 + c] = rbf(sv); }
  }
}

__global__ void __launch_bounds__(512) k2(const u16* kc, const float* q, float* sc) {
  const int bid = blockIdx.x, kvh = bid & 7, b = (bid >> 3) & 7, s = bid >> 6;
  const int tid = threadIdx.x, lane = tid & 63, wave = tid >> 6;
  const int c = lane & 31, u = lane >> 5;
  float qf[2][8];
  for (int hh = 0; hh < 2; ++hh) for (int j = 0; j < 8; ++j) qf[hh][j] = q[b * 4096 + (kvh * 2 + hh) * 256 + c * 8 + j];
  const u16* kb = kc + ((size_t)(b * HKV + kvh) * SK + (SK - W) + s * 256 + wave * 32) * DH;
  u32x4 kv[16];
#pragma unroll
  for (int t = 0; t < 16; ++t) kv[t] = *(const u32x4*)(kb + (size_t)(2 * t + u) * DH + c * 8);
#pragma unroll
  for (int t = 0; t < 16; ++t) {
    u32x4 v = kv[t];
    float kf[8] = {lo(v.x), hi(v.x), lo(v.y), hi(v.y), lo(v.z), hi(v.z), lo(v.w), hi(v.w)};
    float d0 = 0.f, d1 = 0.f;
#pragma unroll
    for (int j = 0; j < 8; ++j) { d0 += kf[j] * qf[0][j]; d1 += kf[j] * qf[1][j]; }
    for (int o = 16; o >= 1; o >>= 1) { d0 += __shfl_xor(d0, o); d1 += __shfl_xor(d1, o); }
    if (c == 0) {
      const int key = s * 256 + wave * 32 + 2 * t + u;
      if (MIMIC) { d0 = rbf(d0); d1 = rbf(d1); }
      sc[(b * HQ + kvh * 2 + 0) * W + key] = d0 * 0.0625f;
      sc[(b * HQ + kvh * 2 + 1) * W + key] = d1 * 0.0625f;
    }
  }
}

__global__ void __launch_bounds__(512) k3(const u16* vc, const float* sc, float* o) {
  __shared__ float p[2][W];
  __shared__ float red[2][8];
  __shared__ float ored[64][2][64];
  const int bid = blockIdx.x, kvh = bid & 7, b = (bid >> 3) & 7, s = bid >> 6;
  const int tid = threadIdx.x, lane = tid & 63, wave = tid >> 6;
  const int c = lane & 7, u = lane >> 3;
  const u16* vb = vc + ((size_t)(b * HKV + kvh) * SK + (SK - W) + wave * 128) * DH + s * 64;
  u32x4 vv[16];
#pragma unroll
  for (int t = 0; t < 16; ++t) vv[t] = *(const u32x4*)(vb + (size_t)(8 * t + u) * DH + c * 8);
  float sv[2][2], m[2];
  for (int hh = 0; hh < 2; ++hh) {
    sv[hh][0] = sc[(b * HQ + kvh * 2 + hh) * W + tid]; sv[hh][1] = sc[(b * HQ + kvh * 2 + hh) * W + tid + 512];
    float mm = fmaxf(sv[hh][0], sv[hh][1]);
    for (int o2 = 32; o2 >= 1; o2 >>= 1) mm = fmaxf(mm, __shfl_xor(mm, o2));
    if (lane == 0) red[hh][wave] = mm;
  }
  __syncthreads();
  for (int hh = 0; hh < 2; ++hh) { float mm = red[hh][0]; for (int i = 1; i < 8; ++i) mm = fmaxf(mm, red[hh][i]); m[hh] = mm; }
  __syncthreads();
  for (int hh = 0; hh < 2; ++hh) {
    sv[hh][0] = __expf(sv[hh][0] - m[hh]); sv[hh][1] = __expf(sv[hh][1] - m[hh]);
    float ss = sv[hh][0] + sv[hh][1];
    for (int o2 = 32; o2 >= 1; o2 >>= 1) ss += __shfl_xor(ss, o2);
    if (lane == 0) red[hh][wave] = ss;
  }
  __syncthreads();
  for (int hh = 0; hh < 2; ++hh) { float ss = 0; for (int i = 0; i < 8; ++i) ss += red[hh][i];
    float p0 = sv[hh][0] / ss, p1 = sv[hh][1] / ss;
    if (MIMIC) { p0 = rbf(p0); p1 = rbf(p1); }
    p[hh][tid] = p0; p[hh][tid + 512] = p1; }
  __syncthreads();
  float acc[2][8];
  for (int hh = 0; hh < 2; ++hh) for (int j = 0; j < 8; ++j) acc[hh][j] = 0.f;
#pragma unroll
  for (int t = 0; t < 16; ++t) {
    u32x4 v = vv[t];
    float vf[8] = {lo(v.x), hi(v.x), lo(v.y), hi(v.y), lo(v.z), hi(v.z), lo(v.w), hi(v.w)};
    const int key = wave * 128 + 8 * t + u;
    float p0 = p[0][key], p1 = p[1][key];
#pragma unroll
    for (int j = 0; j < 8; ++j) { acc[0][j] += p0 * vf[j]; acc[1][j] += p1 * vf[j]; }
  }
  for (int hh = 0; hh < 2; ++hh) for (int j = 0; j < 8; ++j) ored[wave * 8 + u][hh][c * 8 + j] = acc[hh][j];
  __syncthreads();
  if (tid < 128) {
    const int hh = tid >> 6, d = tid & 63;
    float ss = 0.f;
    for (int g = 0; g < 64; ++g) ss += ored[g][hh][d];
    if (MIMIC) ss = rbf(ss);
    o[b * 4096 + (kvh * 2 + hh) * 256 + s * 64 + d] = ss;
  }
}

__global__ void __launch_bounds__(256) k4(const u16* wo, const u16* w2, const float* o, const float* h, float* part) {
  __shared__ float red[4][8][448];
  const int j = blockIdx.x, tid = threadIdx.x, lane = tid & 63, wave = tid >> 6;
  const bool iso = j < 128;
  const u16* w = iso ? wo + (size_t)j * 32 * D : w2 + (size_t)(j - 128) * 32 * D;
  const float* a = iso ? o + j * 32 : h + (j - 128) * 32;
  const int lda = iso ? 4096 : F;
  float acc[8][8];
  for (int b = 0; b < 8; ++b) for (int c = 0; c < 8; ++c) acc[b][c] = 0.f;
  if (lane < 56) {
    for (int r = wave * 8; r < wave * 8 + 8; ++r) {
      u32x4 v = *(const u32x4*)(w + (size_t)r * D + lane * 8);
      float wf[8] = {lo(v.x), hi(v.x), lo(v.y), hi(v.y), lo(v.z), hi(v.z), lo(v.w), hi(v.w)};
#pragma unroll
      for (int b = 0; b < 8; ++b) { float av = a[b * lda + r];
#pragma unroll
        for (int c = 0; c < 8; ++c) acc[b][c] += av * wf[c]; }
    }
    for (int b = 0; b < 8; ++b) for (int c = 0; c < 8; ++c) red[wave][b][lane * 8 + c] = acc[b][c];
  }
  __syncthreads();
  for (int i = tid; i < 8 * 448; i += 256) {
    const int b = i / 448, c = i % 448;
    part[(size_t)j * 3584 + i] = red[0][b][c] + red[1][b][c] + red[2][b][c] + red[3][b][c];
  }
}

__global__ void __launch_bounds__(256) k5(const u16* x, const u16* gw, const float* part, u16* out) {
  __shared__ float ra[4][64], rb[4][64];
  __shared__ float gs[4];
  const int b = blockIdx.x / 7, cg = blockIdx.x % 7, tid = threadIdx.x, lane = tid & 63, wave = tid >> 6;
  float sa = 0.f, sb = 0.f;
  for (int j = wave; j < 128; j += 4) sa += part[(size_t)j * 3584 + b * 448 + cg * 64 + lane];
  for (int j = 128 + wave; j < 184; j += 4) sb += part[(size_t)j * 3584 + b * 448 + cg * 64 + lane];
  ra[wave][lane] = sa; rb[wave][lane] = sb;
  float g = 0.f;
  for (int d = tid; d < D; d += 256) g += rbf(bf2f(x[b * D + d]) * bf2f(gw[d]));
  for (int o2 = 32; o2 >= 1; o2 >>= 1) g += __shfl_xor(g, o2);
  if (lane == 0) gs[wave] = g;
  __syncthreads();
  if (tid < 64) {
    float a = ra[0][lane] + ra[1][lane] + ra[2][lane] + ra[3][lane];
    float bb = rb[0][lane] + rb[1][lane] + rb[2][lane] + rb[3][lane];
    float gg = rbf(gs[0] + gs[1] + gs[2] + gs[3]);
    gg = rbf(1.f / (1.f + __expf(-gg)));
    float r;
    if (MIMIC) { a = rbf(a); bb = rbf(bb); r = rbf(gg * a) + rbf(rbf(1.f - gg) * bb); }
    else r = gg * a + (1.f - gg) * bb;
    out[b * D + cg * 64 + lane] = f2bf(r);
  }
}

extern "C" void kernel_launch(void* const* d_in, const int* in_sizes, int n_in, void* d_out, int out_size, void* d_ws, size_t ws_size, hipStream_t stream) {
  const u16* x = (const u16*)d_in[0]; const u16* kc = (const u16*)d_in[1]; const u16* vc = (const u16*)d_in[2];
  const u16* wq = (const u16*)d_in[3]; const u16* wo = (const u16*)d_in[4];
  const u16* w1 = (const u16*)d_in[6]; const u16* w2 = (const u16*)d_in[7]; const u16* gw = (const u16*)d_in[8];
  float* ws = (float*)d_ws;
  float* q = ws; float* h = q + 8 * 4096; float* sc = h + 8 * F; float* o = sc + 8 * 16 * W; float* part = o + 8 * 4096;
  k1<<<128 + 56, 256, 0, stream>>>(x, wq, w1, q, h);
  k2<<<256, 512, 0, stream>>>(kc, q, sc);
  k3<<<256, 512, 0, stream>>>(vc, sc, o);
  k4<<<184, 256, 0, stream>>>(wo, w2, o, h, part);
  k5<<<56, 256, 0, stream>>>(x, gw, part, (u16*)d_out);
}
```

```cpp
#include <hip/hip_runtime.h>
#include <stdint.h>

#define KW 3
#define VPRE 4
#define WW 2
#define MAPB 0

typedef unsigned short u16;
typedef unsigned u32;
typedef u32 u32x2 __attribute__((ext_vector_type(2)));
typedef u32 u32x4 __attribute__((ext_vector_type(4)));
typedef float f32x2 __attribute__((ext_vector_type(2)));
typedef float f32x4 __attribute__((ext_vector_type(4)));
typedef short s16x4 __attribute__((ext_vector_type(4)));
typedef short s16x8 __attribute__((ext_vector_type(8)));
typedef __bf16 bf16x2 __attribute__((ext_vector_type(2)));
typedef __bf16 bf16x8 __attribute__((ext_vector_type(8)));
#define LAS __attribute__((address_space(3)))
#define GAS __attribute__((address_space(1)))
#define SB() __builtin_amdgcn_sched_barrier(0)
#define USE(a) asm volatile("" ::"v"(a))
#define PIN(a) asm volatile("" : "+v"(a))
#define LGKM0() asm volatile("s_waitcnt lgkmcnt(0)" ::: "memory")
#define VM0() asm volatile("s_waitcnt vmcnt(0)" ::: "memory")
#define BAR() do { asm volatile("s_waitcnt lgkmcnt(0)" ::: "memory"); __builtin_amdgcn_s_barrier(); asm volatile("" ::: "memory"); } while (0)

constexpr int D = 448, NQ = 4096, F = 1792;
constexpr int AUX_NT = 2, AUX_SC1 = 16, RS3 = 0x00020000;
constexpr u32 EP_OFF = 0;
constexpr u32 QG_OFF = 4096;
constexpr u32 HG_OFF = QG_OFF + 2 * 8 * NQ * 8;
constexpr u32 SG_OFF = HG_OFF + 2 * 8 * F * 8;
constexpr u32 YG_OFF = SG_OFF + 64 * 1024 * 8;
constexpr u32 WS_END = YG_OFF + 8 * 32 * 32 * 16 * 8;
constexpr u32 ST_OFF = 4u << 20;
constexpr int L_WA = 0, L_QS = 28672, L_HS = L_QS + 1024, L_PS = L_HS + 256, L_OP = L_PS + 4096, L_YP = L_OP + 4096,
              L_FL = L_YP + 8 * 448 * 4, L_END = L_FL + 64;
constexpr u32 SPIN_G = 1u << 15, SPIN_L = 1u << 20;

__device__ __forceinline__ float bflo(u32 v) { return __builtin_bit_cast(float, v << 16); }
__device__ __forceinline__ float bfhi(u32 v) { return __builtin_bit_cast(float, v & 0xffff0000u); }
__device__ __forceinline__ float asf(u32 v) { return __builtin_bit_cast(float, v); }
__device__ __forceinline__ u32 asu(float v) { return __builtin_bit_cast(u32, v); }
__device__ __forceinline__ u32 pkbf(float lo, float hi) { f32x2 p = {lo, hi}; return __builtin_bit_cast(u32, __builtin_convertvector(p, bf16x2)); }
__device__ __forceinline__ float rbf(float f) { return (float)(__bf16)f; }
__device__ __forceinline__ float dot2(u32 a, u32 b, float c) {
  return __builtin_amdgcn_fdot2_f32_bf16(__builtin_bit_cast(bf16x2, a), __builtin_bit_cast(bf16x2, b), c, false);
}
__device__ __forceinline__ f32x4 mfma(u32x4 a, u32x4 b, f32x4 c) {
  return __builtin_amdgcn_mfma_f32_16x16x32_bf16(__builtin_bit_cast(bf16x8, a), __builtin_bit_cast(bf16x8, b), c, 0, 0, 0);
}
template <int CTRL> __device__ __forceinline__ float dpp(float x) {
  return __builtin_bit_cast(float, __builtin_amdgcn_mov_dpp(__builtin_bit_cast(int, x), CTRL, 0xf, 0xf, true));
}
constexpr int XOR1 = 0xB1, XOR2 = 0x4E, HMIR = 0x141, ROR8 = 0x128, ROR4 = 0x124;
__device__ __forceinline__ float xsum16(float x) { auto s = __builtin_amdgcn_permlane16_swap(asu(x), asu(x), false, false); return asf(s[0]) + asf(s[1]); }
__device__ __forceinline__ float xsum32(float x) { auto s = __builtin_amdgcn_permlane32_swap(asu(x), asu(x), false, false); return asf(s[0]) + asf(s[1]); }
__device__ __forceinline__ float xmax16(float x) { auto s = __builtin_amdgcn_permlane16_swap(asu(x), asu(x), false, false); return fmaxf(asf(s[0]), asf(s[1])); }
__device__ __forceinline__ float xmax32(float x) { auto s = __builtin_amdgcn_permlane32_swap(asu(x), asu(x), false, false); return fmaxf(asf(s[0]), asf(s[1])); }
__device__ __forceinline__ float wave_sum(float x) {
  x += dpp<XOR1>(x); x += dpp<XOR2>(x); x += dpp<HMIR>(x); x += dpp<ROR8>(x); return xsum32(xsum16(x));
}
__device__ __forceinline__ float wave_max(float x) {
  x = fmaxf(x, dpp<XOR1>(x)); x = fmaxf(x, dpp<XOR2>(x)); x = fmaxf(x, dpp<HMIR>(x)); x = fmaxf(x, dpp<ROR8>(x)); return xmax32(xmax16(x));
}
__device__ __forceinline__ __amdgpu_buffer_rsrc_t rsrc(const void* p, u32 bytes) { return __builtin_amdgcn_make_buffer_rsrc((void*)p, 0, (int)bytes, RS3); }
template <int AUX> __device__ __forceinline__ u32x4 ld16(__amdgpu_buffer_rsrc_t r, u32 voff, u32 soff) { return __builtin_amdgcn_raw_buffer_load_b128(r, (int)voff, (int)soff, AUX); }
__device__ __forceinline__ void lds_wait(volatile LAS u32* f, u32 want) {
  for (u32 n = 0; *f < want && n < SPIN_L; ++n) __builtin_amdgcn_s_sleep(1);
  asm volatile("" ::: "memory");
}
__device__ __forceinline__ u32 gpos(u32 c) { return (c & ~31u) | (((c >> 1) & 1u) << 4) | (((c >> 2) & 7u) << 1) | (c & 1u); }

#define STAMP(i)
#define STAMP_OUT()

__global__ void __launch_bounds__(1024, 4)
fused_decode(const u16* __restrict__ x, const char* __restrict__ kc, const char* __restrict__ vc, const char* __restrict__ wq,
             const char* __restrict__ wo, const char* __restrict__ w1, const char* __restrict__ w2, const u16* __restrict__ gw,
             u16* __restrict__ out, char* __restrict__ ws) {
  __shared__ __attribute__((aligned(16))) char smem[L_END];
  LAS char* L = (LAS char*)smem;
  volatile LAS u32* FL = (volatile LAS u32*)(L + L_FL);
  const int bid = blockIdx.x, kvh = bid & 7, r = bid >> 3;
  const int b = MAPB ? (r & 7) : (r >> 2), s = MAPB ? (r >> 3) : (r & 3);
  const int lane = threadIdx.x & 63, wv = __builtin_amdgcn_readfirstlane(threadIdx.x >> 6), w = wv & 7;
  asm volatile("" ::"s"(x), "s"(kc), "s"(vc), "s"(wq), "s"(wo), "s"(w1), "s"(w2), "s"(gw), "s"(out), "s"(ws));
  const __amdgpu_buffer_rsrc_t rws = rsrc(ws, WS_END);
  STAMP(0);

  if (wv >= 8) {
    const size_t slab = (size_t)(b * 8 + kvh) * 2097152 + 1572864;
    const __amdgpu_buffer_rsrc_t rk = rsrc(kc + slab, 524288), rv = rsrc(vc + slab, 524288);
    const u32 ko = (w * 4 + s) * 4096 + (lane >> 3) * 512 + (lane & 7) * 16;
    const u32 vo = (w * 64 + 8 * (lane >> 3)) * 512 + s * 128 + (lane & 7) * 16;
    u32x4 k[16], v[16];
    BAR();
    STAMP(1);
#pragma unroll
    for (int j = 0; j < 16; ++j) {
      k[j] = ld16<AUX_NT>(rk, ko, (j >> 2) * 131072 + (j & 3) * 128); SB();
      if (j >= KW) { USE(k[j - KW]); SB(); }
    }
#pragma unroll
    for (int j = 0; j < VPRE; ++j) { v[j] = ld16<AUX_NT>(rv, vo, (j >> 3) * 262144 + (j & 7) * 512); SB(); }
    STAMP(2);
    lds_wait(FL + 0, 1u);
    const u32 tag = FL[3];
    float sc[4][2];
#pragma unroll
    for (int hp = 0; hp < 2; ++hp) {
      sc[2 * hp][0] = sc[2 * hp][1] = sc[2 * hp + 1][0] = sc[2 * hp + 1][1] = 0.f;
#pragma unroll
      for (int t = 0; t < 4; ++t) {
        const u32x4 q0 = *(const LAS u32x4*)(L + L_QS + t * 128 + (lane & 7) * 16);
        const u32x4 q1 = *(const LAS u32x4*)(L + L_QS + 512 + t * 128 + (lane & 7) * 16);
#pragma unroll
        for (int d = 0; d < 4; ++d)
#pragma unroll
          for (int jj = 2 * hp; jj < 2 * hp + 2; ++jj) {
            sc[jj][0] = dot2(k[4 * jj + t][d], q0[d], sc[jj][0]);
            sc[jj][1] = dot2(k[4 * jj + t][d], q1[d], sc[jj][1]);
          }
      }
      SB();
    }
#pragma unroll
    for (int jj = 0; jj < 4; ++jj)
#pragma unroll
      for (int h = 0; h < 2; ++h) { float t = sc[jj][h]; t += dpp<XOR1>(t); t += dpp<XOR2>(t); t += dpp<HMIR>(t); sc[jj][h] = t; }
    {
      const int jj = lane & 3;
      const float s0 = jj == 0 ? sc[0][0] : jj == 1 ? sc[1][0] : jj == 2 ? sc[2][0] : sc[3][0];
      const float s1 = jj == 0 ? sc[0][1] : jj == 1 ? sc[1][1] : jj == 2 ? sc[2][1] : sc[3][1];
      const u32 key = ((jj * 8 + w) * 4 + s) * 8 + (lane >> 3);
      const u32x2 gr = {pkbf(s0, s1), tag};
      if ((lane & 4) == 0) __builtin_amdgcn_raw_buffer_store_b64(gr, rws, (int)(key * 8), (int)(SG_OFF + (b * 8 + kvh) * 8192), AUX_SC1);
      if (lane == 0) __hip_atomic_fetch_add((LAS u32*)(L + L_FL + 4), 1u, __ATOMIC_RELAXED, __HIP_MEMORY_SCOPE_WORKGROUP);
    }
    STAMP(3);
    u32x4 fr[2][8];
#define VPERM(a_) do { _Pragma("unroll") for (int d = 0; d < 4; ++d) { \
      u32 lo_ = __builtin_amdgcn_perm(v[2 * (a_) + 1][d], v[2 * (a_)][d], 0x05040100u); \
      u32 hi_ = __builtin_amdgcn_perm(v[2 * (a_) + 1][d], v[2 * (a_)][d], 0x07060302u); \
      PIN(lo_); PIN(hi_); fr[(a_) >> 2][2 * d][(a_) & 3] = lo_; fr[(a_) >> 2][2 * d + 1][(a_) & 3] = hi_; } } while (0)
#pragma unroll
    for (int j = VPRE; j < 16; ++j) {
      v[j] = ld16<AUX_NT>(rv, vo, (j >> 3) * 262144 + (j & 7) * 512); SB();
      if (j >= KW) {
        USE(v[j - KW]);
#pragma unroll
        for (int a = 0; a < 8; ++a) if (2 * a + 1 + KW == j || (j == VPRE && 2 * a + 1 + KW < VPRE)) { VPERM(a); }
        SB();
      }
    }
    STAMP(4);
    lds_wait(FL + 2, 1u);
    STAMP(5);
    const int m = lane & 15;
    const u32 po = L_PS + ((m >> 2) & 1) * 2048 + (w * 64 + (lane >> 4) * 16 + (m & 1) * 8) * 2;
    const u32x4 pa0 = *(const LAS u32x4*)(L + po), pa1 = *(const LAS u32x4*)(L + po + 1024);
    f32x4 acc[8];
#pragma unroll
    for (int a = 0; a < 4; ++a) if (2 * a + 1 + KW > 15) { VPERM(a); }
#pragma unroll
    for (int j = 0; j < 8; ++j) acc[j] = mfma(pa0, fr[0][j], f32x4{0.f, 0.f, 0.f, 0.f});
    SB();
#pragma unroll
    for (int a = 4; a < 8; ++a) if (2 * a + 1 + KW > 15) { VPERM(a); }
#pragma unroll
    for (int j = 0; j < 8; ++j) acc[j] = mfma(pa1, fr[1][j], acc[j]);
    float o[8];
#pragma unroll
    for (int j = 0; j < 8; ++j) o[j] = acc[j][0] + dpp<ROR8>(acc[j][1]);
    if ((lane & 0x28) == 0) {
      LAS f32x4* dst = (LAS f32x4*)(L + L_OP + w * 512 + (lane >> 4) * 256 + (lane & 7) * 32);
      dst[0] = f32x4{o[0], o[1], o[2], o[3]}; dst[1] = f32x4{o[4], o[5], o[6], o[7]};
    }
    STAMP(6);
    BAR();
    if (w != 0) { STAMP_OUT(); return; }
    BAR();
    const int j32 = kvh * 4 + s;
    const u32 yb = YG_OFF + (b * 32 + j32) * 4096;
    u32x4 y[4];
    for (u32 n = 0; n < SPIN_G; ++n) {
      asm volatile("" ::: "memory");
#pragma unroll
      for (int i = 0; i < 4; ++i) y[i] = ld16<AUX_SC1>(rws, lane * 16, yb + i * 1024);
      bool ok = true;
#pragma unroll
      for (int i = 0; i < 4; ++i) ok &= (y[i][1] == tag) & (y[i][3] == tag);
      if (__all(ok || (lane & 7) == 7)) break;
    }
    float y0 = (asf(y[0][0]) + asf(y[1][0])) + (asf(y[2][0]) + asf(y[3][0]));
    float y1 = (asf(y[0][2]) + asf(y[1][2])) + (asf(y[2][2]) + asf(y[3][2]));
    y0 += dpp<ROR8>(y0); y1 += dpp<ROR8>(y1);
    y0 = xsum32(xsum16(y0)); y1 = xsum32(xsum16(y1));
    if (lane < 7) *(GAS u32*)(out + b * D + j32 * 14 + lane * 2) = pkbf(y0, y1);
    if (j32 == 0 && lane == 0) __builtin_amdgcn_raw_buffer_store_b32(tag, rws, 0, (int)(EP_OFF + b * 4), AUX_SC1);
    STAMP(7);
    STAMP_OUT();
    return;
  }

  if (w == 0) {
    const int u = b * 3 + s;
    const bool act = s < 3 && u < 23;
    if (act) {
      const bool isq = u < 16;
      const int hu = kvh * 7 + (u - 16);
      const int half = isq ? (u & 1) : (hu & 1);
      const int col0 = isq ? kvh * 512 + (u >> 1) * 64 : (hu >> 1) * 64;
      const u32 ldb = isq ? NQ * 2 : F * 2;
      const char* src = (isq ? wq : w1) + (size_t)(half * 224 + (lane >> 3)) * ldb + col0 * 2;
      const int sw = (lane >> 4) & 1;
      const char* src0 = src + (((lane & 7) ^ (2 * sw)) * 16);
      const char* src1 = src + (((lane & 7) ^ (2 * (sw | 2))) * 16);
#pragma unroll
      for (int i = 0; i < 28; ++i)
        __builtin_amdgcn_global_load_lds((const GAS void*)(((i & 1) ? src1 : src0) + (size_t)i * 8 * ldb), (LAS void*)(L + L_WA + i * 1024), 16, 0, AUX_NT);
      u32x4 xf[7];
#pragma unroll
      for (int st = 0; st < 7; ++st) xf[st] = *(const GAS u32x4*)(x + (lane & 7) * D + half * 224 + st * 32 + (lane >> 4) * 8);
      const u32 ev = __builtin_amdgcn_raw_buffer_load_b32(rws, (lane & 7) * 4, EP_OFF, AUX_SC1);
      SB();
      BAR();
      VM0();
      SB();
      const u32 tg = ev + 1u;
      STAMP(1);
      const int g = lane >> 4, q = (lane >> 2) & 3, p = lane & 3;
      const int swr = (q >> 1) | ((g & 1) << 1);
      f32x4 dd[4];
#pragma unroll
      for (int T = 0; T < 4; ++T) {
        dd[T] = f32x4{0.f, 0.f, 0.f, 0.f};
        LAS char* base = L + L_WA + (8 * g + q) * 128 + ((T ^ swr) * 32) + p * 8;
#pragma unroll
        for (int st = 0; st < 7; ++st) {
          const s16x4 a0 = __builtin_amdgcn_ds_read_tr16_b64_v4i16((LAS s16x4*)(base + st * 4096));
          const s16x4 a1 = __builtin_amdgcn_ds_read_tr16_b64_v4i16((LAS s16x4*)(base + st * 4096 + 512));
          dd[T] = mfma(__builtin_bit_cast(u32x4, __builtin_shufflevector(a0, a1, 0, 1, 2, 3, 4, 5, 6, 7)), xf[st], dd[T]);
        }
      }
      const u32 rowb = isq ? QG_OFF + (half * 8 + (lane & 7)) * (NQ * 8) : HG_OFF + (half * 8 + (lane & 7)) * (F * 8);
      const bool hiT = (lane & 8) != 0;
#pragma unroll
      for (int tp = 0; tp < 2; ++tp) {
        float e[4];
#pragma unroll
        for (int i = 0; i < 4; ++i) { const float lo_ = dd[2 * tp][i], hi_ = dd[2 * tp + 1][i]; e[i] = hiT ? hi_ : lo_; }
        const u32 off = rowb + (col0 + tp * 32) * 8 + ((hiT ? 4 : 0) + g) * 16;
        __builtin_amdgcn_raw_buffer_store_b128(u32x4{asu(e[0]), tg, asu(e[1]), tg}, rws, (int)off, 0, AUX_SC1);
        __builtin_amdgcn_raw_buffer_store_b128(u32x4{asu(e[2]), tg, asu(e[3]), tg}, rws, (int)off, 128, AUX_SC1);
      }
      STAMP(2);
    } else {
      BAR();
    }
  } else if (w == 1) {
    if (lane < 4) FL[lane] = 0u;
    const u32 ev = __builtin_amdgcn_raw_buffer_load_b32(rws, 0, EP_OFF + b * 4, AUX_SC1);
    const int l56 = lane < 56 ? lane : 55;
    const u32x4 xv = *(const GAS u32x4*)(x + b * D + l56 * 8), gv = *(const GAS u32x4*)(gw + l56 * 8);
    SB();
    BAR();
    SB();
    const u32 tag = __builtin_amdgcn_readfirstlane(ev) + 1u;
    float gs = 0.f;
#pragma unroll
    for (int d = 0; d < 4; ++d) gs += rbf(bflo(xv[d]) * bflo(gv[d])) + rbf(bfhi(xv[d]) * bfhi(gv[d]));
    gs = rbf(wave_sum(lane < 56 ? gs : 0.f));
    const float gate = rbf(1.f / (1.f + __expf(-gs)));
    const int hl = (lane & 31) < 28 ? (lane & 31) : 27;
    const u32 hoff = HG_OFF + ((lane >> 5) * 8 + b) * (F * 8) + gpos((kvh * 4 + s) * 56 + 2 * hl) * 8;
    const u32 qb = QG_OFF + b * (NQ * 8) + kvh * 4096;
    u32x4 qa[8], hg;
    STAMP(1);
    for (u32 n = 0; n < SPIN_G; ++n) {
      asm volatile("" ::: "memory");
#pragma unroll
      for (int i = 0; i < 8; ++i) qa[i] = ld16<AUX_SC1>(rws, lane * 16, qb + (i >> 2) * (8 * NQ * 8) + (i & 3) * 1024);
      hg = ld16<AUX_SC1>(rws, hoff, 0);
      bool ok = (hg[1] == tag) & (hg[3] == tag);
#pragma unroll
      for (int i = 0; i < 8; ++i) ok &= (qa[i][1] == tag) & (qa[i][3] == tag);
      if (__all(ok)) break;
      __builtin_amdgcn_s_sleep(4);
    }
    STAMP(2);
#pragma unroll
    for (int i = 0; i < 4; ++i) {
      const u32 pos = 2 * (64 * i + lane);
      const u32 c = (pos & ~31u) | (((pos >> 4) & 1u) << 1) | (((pos >> 1) & 7u) << 2);
      *(LAS u32*)(L + L_QS + c * 2) = pkbf(asf(qa[i][0]) + asf(qa[4 + i][0]), asf(qa[i][2]) + asf(qa[4 + i][2]));
    }
    {
      const float h0 = rbf(xsum32(asf(hg[0]))), h1 = rbf(xsum32(asf(hg[2])));
      const float a0 = rbf(h0 / (1.f + __expf(-h0))), a1 = rbf(h1 / (1.f + __expf(-h1)));
      if (lane < 28) *(LAS f32x2*)(L + L_HS + lane * 8) = f32x2{a0, a1};
      if (lane == 0) { *(LAS f32x2*)(L + L_HS + 224) = f32x2{gate, rbf(1.f - gate)}; FL[3] = tag; }
    }
    LGKM0();
    if (lane == 0) FL[0] = 1u;
  } else {
    BAR();
  }

  const int l56 = lane < 56 ? lane : 55;
  const __amdgpu_buffer_rsrc_t ro = rsrc(wo + (size_t)((kvh * 2 + (w >> 2)) * 256 + s * 64 + (w & 3) * 16) * 896, 16 * 896);
  const __amdgpu_buffer_rsrc_t rf = rsrc(w2 + (size_t)((kvh * 4 + s) * 56 + w * 7) * 896, 7 * 896);
  u32 wp[8][8];
  float af[8];
  {
    u32x4 t[23];
#pragma unroll
    for (int j = 0; j < 23 + WW; ++j) {
      if (j < 23) { t[j] = j < 16 ? ld16<AUX_NT>(ro, l56 * 16, j * 896) : ld16<AUX_NT>(rf, l56 * 16, (j - 16) * 896); SB(); }
      const int c = j - WW;
      if (c >= 0) {
        if (c < 16) {
          USE(t[c]);
          if (c & 1) {
#pragma unroll
            for (int d = 0; d < 4; ++d) {
              wp[c >> 1][2 * d] = __builtin_amdgcn_perm(t[c][d], t[c - 1][d], 0x05040100u);
              wp[c >> 1][2 * d + 1] = __builtin_amdgcn_perm(t[c][d], t[c - 1][d], 0x07060302u);
              PIN(wp[c >> 1][2 * d]); PIN(wp[c >> 1][2 * d + 1]);
            }
          }
        } else {
          if (c == 16) {
            lds_wait(FL + 0, 1u);
#pragma unroll
            for (int d = 0; d < 8; ++d) af[d] = 0.f;
          }
          const float hv = *(const LAS float*)(L + L_HS + (w * 7 + c - 16) * 4);
#pragma unroll
          for (int d = 0; d < 4; ++d) { af[2 * d] = fmaf(hv, bflo(t[c][d]), af[2 * d]); af[2 * d + 1] = fmaf(hv, bfhi(t[c][d]), af[2 * d + 1]); PIN(af[2 * d]); PIN(af[2 * d + 1]); }
        }
        SB();
      }
    }
  }
  const f32x2 gg = *(const LAS f32x2*)(L + L_HS + 224);
  const u32 tag = FL[3];
#pragma unroll
  for (int d = 0; d < 8; ++d) { af[d] *= gg[1]; PIN(af[d]); }
  STAMP(3);

  if (w == 2) {
    lds_wait(FL + 1, 8u);
    const u32 sb = SG_OFF + (b * 8 + kvh) * 8192;
    u32x4 sg[8];
    STAMP(4);
    for (u32 n = 0; n < SPIN_G; ++n) {
      asm volatile("" ::: "memory");
#pragma unroll
      for (int i = 0; i < 8; ++i) sg[i] = ld16<AUX_SC1>(rws, lane * 16, sb + i * 1024);
      bool ok = true;
#pragma unroll
      for (int i = 0; i < 8; ++i) ok &= (sg[i][1] == tag) & (sg[i][3] == tag);
      if (__all(ok)) break;
    }
    STAMP(5);
    constexpr float C2 = 0.0625f * 1.4426950408889634f;
#pragma unroll
    for (int h = 0; h < 2; ++h) {
      float e[16];
#pragma unroll
      for (int i = 0; i < 8; ++i) { e[2 * i] = h ? bfhi(sg[i][0]) : bflo(sg[i][0]); e[2 * i + 1] = h ? bfhi(sg[i][2]) : bflo(sg[i][2]); }
      float mx = e[0];
#pragma unroll
      for (int i = 1; i < 16; ++i) mx = fmaxf(mx, e[i]);
      mx = wave_max(mx);
      float sum = 0.f;
#pragma unroll
      for (int i = 0; i < 16; ++i) { e[i] = __builtin_amdgcn_exp2f((e[i] - mx) * C2); sum += e[i]; }
      const float inv = 1.f / wave_sum(sum);
#pragma unroll
      for (int i = 0; i < 8; ++i) *(LAS u32*)(L + L_PS + h * 2048 + i * 256 + lane * 4) = pkbf(e[2 * i] * inv, e[2 * i + 1] * inv);
    }
    LGKM0();
    if (lane == 0) FL[2] = 1u;
    STAMP(6);
  }

  BAR();
  {
    const LAS float* op = (const LAS float*)(L + L_OP + (lane >> 4) * 1024 + (w * 16 + (lane & 15)) * 4);
    float o = op[0] + op[128];
    o = xsum32(xsum16(o));
    const u32 pr = pkbf(o, dpp<XOR1>(o));
    float ao[8];
#pragma unroll
    for (int d = 0; d < 8; ++d) ao[d] = 0.f;
#pragma unroll
    for (int a = 0; a < 8; ++a) {
      const u32 oa = __builtin_amdgcn_readlane(pr, 2 * a);
#pragma unroll
      for (int d = 0; d < 8; ++d) ao[d] = dot2(wp[a][d], oa, ao[d]);
    }
    if (lane < 56) {
      LAS f32x4* dst = (LAS f32x4*)(L + L_YP + w * 1792 + lane * 32);
      dst[0] = f32x4{fmaf(gg[0], ao[0], af[0]), fmaf(gg[0], ao[1], af[1]), fmaf(gg[0], ao[2], af[2]), fmaf(gg[0], ao[3], af[3])};
      dst[1] = f32x4{fmaf(gg[0], ao[4], af[4]), fmaf(gg[0], ao[5], af[5]), fmaf(gg[0], ao[6], af[6]), fmaf(gg[0], ao[7], af[7])};
    }
  }
  BAR();
  const int tid = threadIdx.x;
  if (tid < 224) {
    f32x2 y = *(const LAS f32x2*)(L + L_YP + tid * 8);
#pragma unroll
    for (int i = 1; i < 8; ++i) y += *(const LAS f32x2*)(L + L_YP + i * 1792 + tid * 8);
    const u32 cj = tid / 7, sl = tid - cj * 7;
    __builtin_amdgcn_raw_buffer_store_b128(u32x4{asu(y[0]), tag, asu(y[1]), tag}, rws, (int)(cj * 4096 + sl * 16),
                                           (int)(YG_OFF + b * 131072 + (kvh * 4 + s) * 128), AUX_SC1);
  }
  STAMP(7);
  STAMP_OUT();
}

extern "C" void kernel_launch(void* const* d_in, const int* in_sizes, int n_in, void* d_out, int out_size, void* d_ws, size_t ws_size,
                              hipStream_t stream) {
  fused_decode<<<256, 1024, 0, stream>>>((const u16*)d_in[0], (const char*)d_in[1], (const char*)d_in[2], (const char*)d_in[3],
                                         (const char*)d_in[4], (const char*)d_in[6], (const char*)d_in[7], (const u16*)d_in[8],
                                         (u16*)d_out, (char*)d_ws);
}
```

```cpp
#include <hip/hip_runtime.h>
#include <stdint.h>

constexpr int T_L = 1044;
constexpr int X_A = 128;
constexpr int D_A = 34;
constexpr int X_B = 236;
constexpr int D_B = 40;
constexpr int W_D0 = 6, W_DT = 8;
constexpr int N_C = 14;
constexpr int T_RG = 450;
constexpr int SKEW_S = 88, SKEW_D = 122;
constexpr int WIN = 10;
constexpr int J_A = 5;

typedef unsigned short u16;
typedef unsigned u32;
typedef u32 u32x2 __attribute__((ext_vector_type(2)));
typedef u32 u32x4 __attribute__((ext_vector_type(4)));
typedef float f32x2 __attribute__((ext_vector_type(2)));
typedef float f32x4 __attribute__((ext_vector_type(4)));
typedef short s16x4 __attribute__((ext_vector_type(4)));
typedef __bf16 bf16x2 __attribute__((ext_vector_type(2)));
typedef __bf16 bf16x8 __attribute__((ext_vector_type(8)));
typedef __amdgpu_buffer_rsrc_t rsrc_t;
#define LAS __attribute__((address_space(3)))
#define GAS __attribute__((address_space(1)))
#define SB() __builtin_amdgcn_sched_barrier(0)
#define USE(a) asm volatile("" ::"v"(a))
#define PIN(a) asm volatile("" : "+v"(a))
#define PINS(a) asm volatile("" : "+s"(a))
#define CLOB() asm volatile("" ::: "memory")
#define VMW(n) asm volatile("s_waitcnt vmcnt(%0)" ::"n"(n) : "memory")
#define BAR() do { asm volatile("s_waitcnt lgkmcnt(0)" ::: "memory"); __builtin_amdgcn_s_barrier(); asm volatile("" ::: "memory"); } while (0)

constexpr int D = 448, NQ = 4096, F = 1792;
constexpr int AUX_NT = 2, AUX_SC1 = 16, RS3 = 0x00020000;
constexpr u32 OOB = 0x80000000u;
constexpr u32 EP_OFF = 0;
constexpr u32 RG_OFF = 128;
constexpr u32 TG_OFF = 2048;
constexpr u32 GG_OFF = TG_OFF + 8 * 2 * 32 * 16;
constexpr u32 QG_OFF = 16384;
constexpr u32 HG_OFF = QG_OFF + 8 * NQ * 24;
constexpr u32 OG_OFF = HG_OFF + 35 * 8192;
constexpr u32 YG_OFF = OG_OFF + 32 * 16384;
constexpr u32 WS_END = YG_OFF + 8 * 28 * 2048;
constexpr int L_WA = 0, L_QS = 28672, L_PS = L_QS + 1024, L_MX = L_PS + 1024, L_WT = L_MX + 64, L_HB = L_WT + 192, L_OA = L_HB + 8 * 144,
              L_FL = L_OA + 8 * 272, L_END = L_FL + 64;
enum { F_A = 0, F_Q, F_M, F_P, F_V, F_R, F_W, F_X, F_C };
constexpr u32 SPIN_G = 1u << 18, SPIN_L = 1u << 23;
constexpr float C2 = 0.0625f * 1.4426950408889634f;

__device__ __forceinline__ float bflo(u32 v) { return __builtin_bit_cast(float, v << 16); }
__device__ __forceinline__ float bfhi(u32 v) { return __builtin_bit_cast(float, v & 0xffff0000u); }
__device__ __forceinline__ float asf(u32 v) { return __builtin_bit_cast(float, v); }
__device__ __forceinline__ u32 asu(float v) { return __builtin_bit_cast(u32, v); }
__device__ __forceinline__ u32 pkbf(float lo, float hi) { f32x2 p = {lo, hi}; return __builtin_bit_cast(u32, __builtin_convertvector(p, bf16x2)); }
__device__ __forceinline__ float rbf(float f) { return (float)(__bf16)f; }
__device__ __forceinline__ float sigm(float f) { return __builtin_amdgcn_rcpf(1.f + __builtin_amdgcn_exp2f(f * -1.4426950408889634f)); }
__device__ __forceinline__ float dot2(u32 a, u32 b, float c) {
  return __builtin_amdgcn_fdot2_f32_bf16(__builtin_bit_cast(bf16x2, a), __builtin_bit_cast(bf16x2, b), c, false);
}
__device__ __forceinline__ f32x4 mfma(u32x4 a, u32x4 b, f32x4 c) {
  return __builtin_amdgcn_mfma_f32_16x16x32_bf16(__builtin_bit_cast(bf16x8, a), __builtin_bit_cast(bf16x8, b), c, 0, 0, 0);
}
template <int CTRL> __device__ __forceinline__ int dppi(int x) { return __builtin_amdgcn_mov_dpp(x, CTRL, 0xf, 0xf, true); }
template <int CTRL> __device__ __forceinline__ float dpp(float x) { return __builtin_bit_cast(float, dppi<CTRL>(__builtin_bit_cast(int, x))); }
constexpr int XOR1 = 0xB1, XOR2 = 0x4E, HMIR = 0x141, ROR8 = 0x128, ROR4 = 0x124;
__device__ __forceinline__ float xsum16(float x) { auto s = __builtin_amdgcn_permlane16_swap(asu(x), asu(x), false, false); return asf(s[0]) + asf(s[1]); }
__device__ __forceinline__ float xsum32(float x) { auto s = __builtin_amdgcn_permlane32_swap(asu(x), asu(x), false, false); return asf(s[0]) + asf(s[1]); }
__device__ __forceinline__ float xmax16(float x) { auto s = __builtin_amdgcn_permlane16_swap(asu(x), asu(x), false, false); return fmaxf(asf(s[0]), asf(s[1])); }
__device__ __forceinline__ float xmax32(float x) { auto s = __builtin_amdgcn_permlane32_swap(asu(x), asu(x), false, false); return fmaxf(asf(s[0]), asf(s[1])); }
__device__ __forceinline__ float wave_sum(float x) {
  x += dpp<XOR1>(x); x += dpp<XOR2>(x); x += dpp<HMIR>(x); x += dpp<ROR8>(x); return xsum32(xsum16(x));
}
__device__ __forceinline__ float wave_max(float x) {
  x = fmaxf(x, dpp<XOR1>(x)); x = fmaxf(x, dpp<XOR2>(x)); x = fmaxf(x, dpp<HMIR>(x)); x = fmaxf(x, dpp<ROR8>(x)); return xmax32(xmax16(x));
}
__device__ __forceinline__ rsrc_t rsrc(const void* p, u32 bytes) { return __builtin_amdgcn_make_buffer_rsrc((void*)p, 0, (int)bytes, RS3); }
template <int AUX> __device__ __forceinline__ u32x4 ld16(rsrc_t r, u32 voff, u32 soff) { return __builtin_amdgcn_raw_buffer_load_b128(r, (int)voff, (int)soff, AUX); }
__device__ __forceinline__ void st16(rsrc_t r, u32 voff, u32 soff, u32 a, u32 b, u32 tag) {
  __builtin_amdgcn_raw_buffer_store_b128(u32x4{a, tag, b, tag}, r, (int)voff, (int)soff, AUX_SC1);
}
__device__ __forceinline__ u32 now32() { return (u32)__builtin_amdgcn_s_memrealtime(); }
__device__ __forceinline__ u32 later(u32 a, u32 b) { return (int)(a - b) < 0 ? b : a; }
__device__ __forceinline__ void until(u32 t) {
#pragma unroll 1
  while ((int)(now32() - t) < 0) {}
}
template <int N, int SLEEP, class U>
__device__ __forceinline__ void gather(rsrc_t rws, u32 voff, u32 soff, u32 tag, u32x4 (&g)[N], U use) {
  CLOB();
#pragma unroll
  for (int i = 0; i < N; ++i) g[i] = ld16<AUX_SC1>(rws, voff + i * 1024, soff);
#pragma unroll 1
  for (u32 n = 0; n < SPIN_G; ++n) {
    u32 bad[N], any = 0;
#pragma unroll
    for (int i = 0; i < N; ++i) { bad[i] = (g[i][1] ^ tag) | (g[i][3] ^ tag); any |= bad[i]; use(i); }
    if (__builtin_expect(__builtin_amdgcn_ballot_w64(any != 0) == 0, 1)) break;
    if (SLEEP) __builtin_amdgcn_s_sleep(SLEEP);
    CLOB();
    u32x4 t[N];
#pragma unroll
    for (int i = 0; i < N; ++i) t[i] = ld16<AUX_SC1>(rws, (bad[i] ? voff : OOB) + i * 1024, soff);
#pragma unroll
    for (int i = 0; i < N; ++i)
#pragma unroll
      for (int d = 0; d < 4; ++d) g[i][d] = bad[i] ? t[i][d] : g[i][d];
  }
}
template <int N, int SLEEP>
__device__ __forceinline__ void gather(rsrc_t rws, u32 voff, u32 soff, u32 tag, u32x4 (&g)[N]) { gather<N, SLEEP>(rws, voff, soff, tag, g, [](int) {}); }
__device__ __forceinline__ void lds_wait(LAS char* L, int f, u32 want) {
  volatile LAS u32* p = (volatile LAS u32*)(L + L_FL) + f;
#pragma unroll 1
  for (u32 n = 0; (u32)__builtin_amdgcn_readfirstlane(*p) < want && n < SPIN_L; ++n) __builtin_amdgcn_s_sleep(1);
  CLOB();
}
__device__ __forceinline__ void lds_post(LAS char* L, int f) {
  asm volatile("s_waitcnt lgkmcnt(0)" ::: "memory");
  __hip_atomic_fetch_add((LAS u32*)(L + L_FL) + f, 1u, __ATOMIC_RELAXED, __HIP_MEMORY_SCOPE_WORKGROUP);
}
__device__ __forceinline__ void dma_rows(const char* base, u32 bytes, u32 so, u32 v0, u32 v1, u32 step, u32 lds_off, u32 npair) {
  const u32x4 rs = {(u32)(uintptr_t)base, (u32)((uintptr_t)base >> 32) & 0xffffu, bytes, (u32)RS3};
  u32 keep;
  asm volatile("s_mov_b32 %0, m0\n\ts_mov_b32 m0, %6\n"
               "1:\n\ts_sub_u32 %2, %2, 1\n\t"
               "buffer_load_dwordx4 %3, %5, %1 offen nt lds\n\t"
               "s_add_u32 m0, m0, 0x400\n\ts_add_u32 %1, %1, %7\n\t"
               "buffer_load_dwordx4 %4, %5, %1 offen nt lds\n\t"
               "s_add_u32 m0, m0, 0x400\n\ts_add_u32 %1, %1, %7\n\t"
               "s_cmp_lg_u32 %2, 0\n\ts_cbranch_scc1 1b\n\t"
               "s_mov_b32 m0, %0"
               : "=&s"(keep), "+&s"(so), "+&s"(npair) : "v"(v0), "v"(v1), "s"(rs), "s"(lds_off), "s"(step) : "memory", "scc");
}
__device__ __forceinline__ u32x4 ldT(LAS char* p) {
  const s16x4 a0 = __builtin_amdgcn_ds_read_tr16_b64_v4i16((LAS s16x4*)p);
  const s16x4 a1 = __builtin_amdgcn_ds_read_tr16_b64_v4i16((LAS s16x4*)(p + 512));
  return __builtin_bit_cast(u32x4, __builtin_shufflevector(a0, a1, 0, 1, 2, 3, 4, 5, 6, 7));
}

#define PACE() do { thr += tau; until(tb + (thr >> 4)); SB(); } while (0)

__global__ void __launch_bounds__(512, 2)
fused_decode(const u16*, const char*, const char*, const char*, const char*, const char*, const char*, const u16*, u16*, char*) {
  typedef unsigned long long u64x8 __attribute__((ext_vector_type(8)));
  typedef unsigned long long u64x2 __attribute__((ext_vector_type(2)));
  u64x8 ka;
  u64x2 kb2;
  unsigned long long tstart;
  asm volatile("s_load_dwordx16 %0, %3, 0x0\n\ts_load_dwordx4 %1, %3, 0x40\n\ts_memrealtime %2"
               : "=&s"(ka), "=&s"(kb2), "=&s"(tstart) : "s"(__builtin_amdgcn_kernarg_segment_ptr()));
  __shared__ __attribute__((aligned(16))) char smem[L_END];
  LAS char* L = (LAS char*)smem;
  const u32 lbase = (u32)(uintptr_t)L;
  const int bid = blockIdx.x, kvh = bid & 7, r = bid >> 3, b = r >> 2, s = r & 3;
  const int lane = threadIdx.x & 63, wv = __builtin_amdgcn_readfirstlane(threadIdx.x >> 6), w = wv & 3;
  const char* pc0;
  u32 code_bytes;
  asm volatile("s_getpc_b64 %0\n1:\n\ts_mov_b32 %1, .Lfd_code_end-1b" : "=s"(pc0), "=s"(code_bytes));
  const u32 cv = __builtin_amdgcn_raw_buffer_load_b32(rsrc(pc0, code_bytes), wv == 4 && lane < 8 ? r * 1024 + lane * 128 : OOB, 0, 0);
  SB();
  const int g4 = lane >> 4, j32 = kvh * 4 + s;
  const int tro = (8 * g4 + ((lane >> 2) & 3)) * 128 + (lane & 3) * 8;
  const int trs = ((lane >> 3) & 1) | ((g4 & 1) << 1);
  const int dsw0 = ((lane & 7) ^ (2 * (g4 & 1))) * 16, dsw1 = ((lane & 7) ^ (2 * (g4 & 1) + 4)) * 16;
  const bool isq = r < 14 || b == 7, four = b == 7 || (kvh < 7 && r >= 19), proj = r < (kvh < 7 ? 23 : 21) || b == 7;
  const int nu = kvh * 5 + r - 14, part = four ? (b == 7 ? s : r - 19) : (isq ? r : nu) & 1;
  const int blk = four ? (isq ? 3 : kvh * 4 + 3) : isq ? (r >> 1) + (r >= 6) : (nu >> 1) + (nu >> 1) / 3;
  const int nst = four ? 4 - (part >> 1) : 7, row0 = four ? part * 128 - (part == 3 ? 32 : 0) : part * 224;
  const int pp0 = (nst * w + 1) >> 1;
  u32 slab = (u32)(b * 8 + kvh) * 2097152u + 1572864u;
  u32 ko = s * 32768 + w * 4096 + (lane >> 3) * 512 + (lane & 7) * 16, kb = wv < 4 ? 524288 : 0;
  u32 ldb = isq ? NQ * 2 : F * 2;
  u32 pso = (row0 + pp0 * 16) * ldb + (isq ? kvh * 1024 : 0) + blk * 128, pv0 = (lane >> 3) * ldb + dsw0, pv1 = (lane >> 3) * ldb + dsw1;
  u32 pn = proj ? ((nst * (w + 1) + 1) >> 1) - pp0 : 0, pl = lbase + L_WA + pp0 * 2048;
  PINS(slab); PIN(ko); PINS(kb); PINS(ldb); PINS(pso); PIN(pv0); PIN(pv1); PINS(pn); PINS(pl);
  SB();
  asm volatile("s_waitcnt lgkmcnt(0)" : "+s"(ka), "+s"(kb2), "+s"(tstart));
  const u32 t0 = (u32)tstart;
  const u16* x = (const u16*)ka[0];
  const char *kc = (const char*)ka[1], *vc = (const char*)ka[2], *wq = (const char*)ka[3], *wo = (const char*)ka[4];
  const char *w1 = (const char*)ka[5], *w2 = (const char*)ka[6];
  const u16* gw = (const u16*)ka[7];
  u16* out = (u16*)kb2[0];
  char* ws = (char*)kb2[1];
  const rsrc_t rws = rsrc(ws, WS_END);
  const rsrc_t rk = rsrc(kc + slab, kb);
  u32x4 k[32];
  k[0] = ld16<AUX_NT>(rk, ko, 0);
  k[1] = ld16<AUX_NT>(rk, ko, 128);
  SB();
  if (wv < 4 && pn) dma_rows(isq ? wq : w1, D * ldb, pso, pv0, pv1, 8 * ldb, pl, pn);
  const u32 ev = __builtin_amdgcn_raw_buffer_load_b32(rws, 0, EP_OFF, AUX_SC1);
  SB();
  const u32 tau = proj ? T_L * 16 / 70 : T_L * 16 / 63;
  if (wv >= 4) {
    if (wv == 4 && lane < 16) *((LAS u32*)(L + L_FL) + lane) = 0u;
    BAR();
  }

  if (__builtin_expect(wv < 4, 1)) {
    const u32 tb = t0;
    u32 thr = tau * (proj ? 8 : 1);
    BAR();
#pragma unroll
    for (int j = 2; j < 32; ++j) {
      PACE();
      k[j] = ld16<AUX_NT>(rk, ko, (j >> 3) * 131072 + ((j >> 2) & 1) * 16384 + (j & 3) * 128); SB();
      if (j == J_A) { VMW(J_A - 1); lds_post(L, F_A); SB(); }
      else VMW(WIN);
      if ((j == N_C - 7 && proj) || (j == N_C - 2 && !proj)) { lds_post(L, F_C); SB(); }
    }
    BAR();
    USE(cv);
    const u32 tag = __builtin_amdgcn_readfirstlane(ev) + 1u;
    const u32 uc = (lane & 48) * 4 + ((lane >> 3) & 1) * 4 + (lane & 7) * 8;
    {
      const u32 qo = QG_OFF + (b * 8 + kvh) * 12288 + w * 3072;
      float q0, q1;
      if (w == 1) {
        u32x4 qg[3];
        gather<3, 16>(rws, lane * 16, qo, tag, qg);
        const float x0 = xsum32(asf(qg[2][0])), x1 = xsum32(asf(qg[2][2]));
        q0 = asf(qg[0][0]) + asf(qg[1][0]) + (lane < 32 ? 0.f : x0);
        q1 = asf(qg[0][2]) + asf(qg[1][2]) + (lane < 32 ? 0.f : x1);
      } else {
        u32x4 qg[2];
        gather<2, 16>(rws, lane * 16, qo, tag, qg);
        q0 = asf(qg[0][0]) + asf(qg[1][0]);
        q1 = asf(qg[0][2]) + asf(qg[1][2]);
      }
      *(LAS u32*)(L + L_QS + w * 256 + uc) = pkbf(q0, q1);
    }
    lds_post(L, F_Q);
    lds_wait(L, F_Q, 4 * 64);
    float sc[2][8];
    {
      u32x4 q[2][4];
#pragma unroll
      for (int t = 0; t < 4; ++t) {
        q[0][t] = *(const LAS u32x4*)(L + L_QS + t * 128 + (lane & 7) * 16);
        q[1][t] = *(const LAS u32x4*)(L + L_QS + 512 + t * 128 + (lane & 7) * 16);
      }
#pragma unroll
      for (int i = 0; i < 8; ++i) sc[0][i] = sc[1][i] = 0.f;
#pragma unroll
      for (int t = 0; t < 4; ++t)
#pragma unroll
        for (int d = 0; d < 4; ++d)
#pragma unroll
          for (int i = 0; i < 8; ++i) {
            sc[0][i] = dot2(k[4 * i + t][d], q[0][t][d], sc[0][i]);
            sc[1][i] = dot2(k[4 * i + t][d], q[1][t][d], sc[1][i]);
          }
    }
#define TR_STEP(HALF, CTRL) _Pragma("unroll") for (int h = 0; h < 2; ++h) _Pragma("unroll") for (int i = 0; i < (HALF); ++i) { \
      const float lo_ = sc[h][i], hi_ = sc[h][i + (HALF)]; const float t1_ = lo_ + dpp<CTRL>(lo_), t2_ = hi_ + dpp<CTRL>(hi_); \
      sc[h][i] = (lane & (HALF)) ? t2_ : t1_; }
    TR_STEP(4, HMIR) TR_STEP(2, XOR2) TR_STEP(1, XOR1)
#undef TR_STEP
    const float s0 = rbf(sc[0][0]), s1 = rbf(sc[1][0]);
    {
      const float m0 = wave_max(s0), m1 = wave_max(s1);
      if (lane == 0) { *(LAS float*)(L + L_MX + w * 4) = m0; *(LAS float*)(L + L_MX + 16 + w * 4) = m1; }
    }
    lds_post(L, F_M);
    lds_wait(L, F_M, 4 * 64);
    const f32x4 ma = *(const LAS f32x4*)(L + L_MX), mb = *(const LAS f32x4*)(L + L_MX + 16);
    const float M0 = fmaxf(fmaxf(ma[0], ma[1]), fmaxf(ma[2], ma[3])), M1 = fmaxf(fmaxf(mb[0], mb[1]), fmaxf(mb[2], mb[3]));
    {
      const float e0 = __builtin_amdgcn_exp2f((s0 - M0) * C2), e1 = __builtin_amdgcn_exp2f((s1 - M1) * C2);
      const u32 pk = pkbf(e0, e1);
      const int lk = 64 * ((lane & 7) >> 1) + 8 * ((lane & 1) * 4 + w) + (lane >> 3);
      *(LAS u16*)(L + L_PS + lk * 2) = (u16)pk;
      *(LAS u16*)(L + L_PS + 512 + lk * 2) = (u16)(pk >> 16);
      const float l0 = wave_sum(e0), l1 = wave_sum(e1);
      if (lane == 0) { *(LAS float*)(L + L_MX + 32 + w * 4) = l0; *(LAS float*)(L + L_MX + 48 + w * 4) = l1; }
    }
    lds_post(L, F_P);
    if (w == 0) {
      lds_wait(L, F_P, 4 * 64);
      const f32x4 la = *(const LAS f32x4*)(L + L_MX + 32 + (lane & 1) * 16);
      const float l = (la[0] + la[1]) + (la[2] + la[3]);
      st16(rws, lane < 2 ? lane * 512 : OOB, TG_OFF + kvh * 1024 + r * 16, asu(lane ? M1 : M0), asu(l), tag);
    }
    if (b == 7) return;
    if (w == 1) {
      lds_wait(L, F_V, 4 * 64);
      u32x4 tt[1];
      gather<1, 16>(rws, lane < 32 ? TG_OFF + kvh * 1024 + (s >> 1) * 512 + lane * 16 : GG_OFF + kvh * 128 + ((lane >> 2) & 7) * 16, 0, tag, tt);
      const float m = asf(tt[0][0]), l = asf(tt[0][2]);
      float M = fmaxf(m, dpp<XOR1>(m)); M = fmaxf(M, dpp<XOR2>(M));
      const float e = __builtin_amdgcn_exp2f((m - M) * C2);
      float den = e * l; den += dpp<XOR1>(den); den += dpp<XOR2>(den);
      auto gsw = __builtin_amdgcn_permlane32_swap(tt[0][0], tt[0][0], false, false);
      if (lane < 32) *(LAS float*)(L + L_WT + lane * 4) = e * asf(gsw[1]) * __builtin_amdgcn_rcpf(den);
      else if ((lane & 3) == 0) *(LAS float*)(L + L_WT + 128 + ((lane >> 2) & 7) * 4) = l;
      lds_post(L, F_W);
      lds_post(L, F_X);
    } else if (w == 2) {
      if (kvh < 7) {
        u32x4 hg[8];
        float ha[8], hc[8];
        gather<8, 16>(rws, lane * 16, HG_OFF + j32 * 8192, tag, hg);
#pragma unroll
        for (int i = 0; i < 8; ++i) { ha[i] = asf(hg[i][0]); hc[i] = asf(hg[i][2]); }
        if (s == 3) {
          gather<8, 16>(rws, lane * 16, HG_OFF + (28 + kvh) * 8192, tag, hg);
#pragma unroll
          for (int i = 0; i < 8; ++i) { ha[i] += asf(hg[i][0]); hc[i] += asf(hg[i][2]); }
        }
#pragma unroll
        for (int i = 0; i < 8; ++i) {
          const float h0 = rbf(xsum32(ha[i])), h1 = rbf(xsum32(hc[i]));
          if (lane < 32) *(LAS u32*)(L + L_HB + i * 144 + uc) = pkbf(h0 * sigm(h0), h1 * sigm(h1));
        }
      }
      lds_post(L, F_W);
    } else if (w == 3) {
      const u32 ro = (lane >> 3) * 896;
      lds_wait(L, F_V, 4 * 64);
      u32 tw = now32() + W_D0;
#pragma unroll
      for (int i = 0; i < 6; ++i) {
        until(tw);
        tw += W_DT;
        if (i < 2) { if (kvh < 7) dma_rows(w2 + b * 128, F * 896 - b * 128, (j32 * 64 + i * 32) * 896, ro + dsw0, ro + dsw1, 8 * 896, lbase + L_WA + 16384 + i * 4096, 2); }
        else dma_rows(wo + b * 128, NQ * 896 - b * 128, (j32 * 128 + (i - 2) * 32) * 896, ro + dsw0, ro + dsw1, 8 * 896, lbase + L_WA + (i - 2) * 4096, 2);
      }
      VMW(8);
      lds_post(L, F_W);
    }
    lds_wait(L, F_W, 3 * 64);
    u32 tp = L_WA + tro + ((w ^ trs) * 32);
    u32x4 wa[4];
    wa[0] = ldT(L + tp); wa[1] = ldT(L + tp + 4096);
    f32x4 df = {0.f, 0.f, 0.f, 0.f};
    if (kvh < 7) {
      const LAS char* hb = L + L_HB + (lane & 7) * 144 + g4 * 16;
      df = mfma(ldT(L + tp + 4 * 4096), *(const LAS u32x4*)hb, df);
      df = mfma(ldT(L + tp + 5 * 4096), *(const LAS u32x4*)(hb + 64), df);
      df *= *(const LAS float*)(L + L_WT + 128 + (lane & 7) * 4);
    }
    u32 yo = (lane & 8) ? OOB : (lane & 7) * (28 * 2048) + g4 * 16, ys = YG_OFF + (b * 4 + w) * 2048 + j32 * 64;
    u32 oo = L_OA + (lane & 7) * 272 + g4 * 16;
    PIN(df); PIN(yo); PIN(oo); PIN(tp); PINS(ys); PIN(wa[0]); PIN(wa[1]);
    if (w == 3) VMW(0);
    BAR();
    u32x4 oa[4];
#pragma unroll
    for (int ks = 0; ks < 4; ++ks) {
      oa[ks] = *(const LAS u32x4*)(L + oo + ks * 64);
      if (ks >= 2) wa[ks] = ldT(L + tp + ks * 4096);
    }
    SB();
    f32x4 d1 = mfma(wa[1], oa[1], f32x4{0.f, 0.f, 0.f, 0.f});
    df = mfma(wa[0], oa[0], df);
    d1 = mfma(wa[2], oa[2], d1);
    df = mfma(wa[3], oa[3], df);
    st16(rws, yo, ys, pkbf(df[0] + d1[0], df[1] + d1[1]), pkbf(df[2] + d1[2], df[3] + d1[3]), tag);
    return;
  }

  u32 tag;
#define GET_TAG() do { SB(); tag = __builtin_amdgcn_readfirstlane(ev) + 1u; PINS(tag); SB(); } while (0)
  if ((w & 1) == 0) {
    if (!proj) GET_TAG();
    else {
      u32x4 xf[7];
#pragma unroll
      for (int st = 0; st < 7; ++st) xf[st] = ld16<0>(rsrc(x, 8 * D * 2), st < nst ? ((lane & 7) * D + row0 + g4 * 8) * 2 : OOB, st * 64);
      GET_TAG();
      lds_wait(L, F_A, 4 * 64);
      f32x4 da = {0.f, 0.f, 0.f, 0.f}, db = {0.f, 0.f, 0.f, 0.f};
      LAS char* ta = L + L_WA + tro + ((w ^ trs) * 32);
      LAS char* tc = L + L_WA + tro + (((w + 1) ^ trs) * 32);
#pragma unroll
      for (int st = 0; st < 7; ++st)
        if (st < 3 || st < nst) {
          da = mfma(ldT(ta + st * 4096), xf[st], da);
          db = mfma(ldT(tc + st * 4096), xf[st], db);
        }
      float e[4];
#pragma unroll
      for (int i = 0; i < 4; ++i) { const float lo_ = da[i], hi_ = db[i]; e[i] = (lane & 8) ? hi_ : lo_; }
      const u32 vo_ = (lane & 7) * (isq ? 98304 : 1024) + (lane & 8) * 8 + g4 * 16;
      const u32 so_ = w * 128 + (isq ? QG_OFF + kvh * 12288 + (blk >> 1) * 3072 + (part < 2 ? part * 1024 + (blk & 1) * 512 : 1024 + part * 512)
                                      : HG_OFF + (part < 2 ? blk : 28 + kvh) * 8192 + (part & 1) * 512);
      st16(rws, vo_, so_, asu(e[0]), asu(e[1]), tag);
      st16(rws, vo_ + 128, so_, asu(e[2]), asu(e[3]), tag);
    }
  } else if (w == 1) {
    if (s != 0) GET_TAG();
    else {
      const int l56 = lane < 56 ? lane : 55;
      const u32x4 xv = *(const GAS u32x4*)(x + b * D + l56 * 8), gv = *(const GAS u32x4*)(gw + l56 * 8);
      GET_TAG();
      float gs = 0.f;
#pragma unroll
      for (int d = 0; d < 4; ++d) gs += rbf(bflo(xv[d]) * bflo(gv[d])) + rbf(bfhi(xv[d]) * bfhi(gv[d]));
      gs = rbf(wave_sum(lane < 56 ? gs : 0.f));
      const float gate = rbf(sigm(gs));
      st16(rws, lane == 0 ? 0 : OOB, GG_OFF + kvh * 128 + b * 16, asu(gate), asu(rbf(1.f - gate)), tag);
    }
    until(t0 + T_RG);
    u32x4 rg[1];
    gather<1, 16>(rws, lane * 16, RG_OFF, tag, rg);
    const float ds = (float)(int)(rg[0][0] - t0), dl = (float)rg[0][2];
    const float mean = wave_sum(ds + 1.35f * dl) * (1.f / 64.f), last = fmaxf(wave_max(ds) - SKEW_S, wave_max(ds + dl) - SKEW_D);
    if (lane == 0) *((LAS u32*)(L + L_FL) + F_R) = t0 + (u32)(((int)fminf(fmaxf(fmaxf(mean, last), -300.f), 300.f) + 303) / 4 * 4 - 300);
  } else {
    const char* pt = lane < 2 ? wo + (size_t)(j32 * 128 + lane * 127) * 896 : lane < 4 ? w2 + (size_t)((kvh < 7 ? j32 * 64 : 0) + (lane - 2) * 63) * 896
                   : lane == 4 ? vc + slab : (const char*)out;
    {
      u32 d0, d1, d2, d3;
      asm volatile("s_load_dword %0, %4, 0x0\n\ts_load_dword %1, %5, 0x0\n\ts_load_dword %2, %6, 0x0\n\ts_load_dword %3, %7, 0x0\n\ts_waitcnt lgkmcnt(0)"
                   : "=&s"(d0), "=&s"(d1), "=&s"(d2), "=&s"(d3)
                   : "s"(vc + slab), "s"(wo + (size_t)j32 * 128 * 896), "s"(w2 + (size_t)j32 * 56 * 896), "s"(out) : "memory");
    }
    GET_TAG();
    lds_wait(L, F_C, 4 * 64);
    st16(rws, s == (b & 3) && lane == 0 ? 0 : OOB, RG_OFF + kvh * 128 + b * 16, t0,
         (u32)max((int)(now32() - t0 - ((proj ? N_C : N_C - 2) * tau >> 4)) - 22, 0), tag);
    const u32 pv = __builtin_nontemporal_load((const GAS u32*)pt);
    USE(pv);
  }
  const rsrc_t rv = rsrc(vc + slab, 524288);
  const u32 vo = s * 32768 + (lane >> 3) * 4096 + w * 128 + (lane & 7) * 16;
  u32x4 v[32], fr[4][8];
#define VPERM1(a_, d) do { \
      u32 lo_ = __builtin_amdgcn_perm(v[2 * (a_) + 1][d], v[2 * (a_)][d], 0x05040100u); \
      u32 hi_ = __builtin_amdgcn_perm(v[2 * (a_) + 1][d], v[2 * (a_)][d], 0x07060302u); \
      PIN(lo_); PIN(hi_); fr[(a_) >> 2][2 * (d)][(a_) & 3] = lo_; fr[(a_) >> 2][2 * (d) + 1][(a_) & 3] = hi_; } while (0)
#define VPERM(a_) do { VPERM1(a_, 0); VPERM1(a_, 1); VPERM1(a_, 2); VPERM1(a_, 3); } while (0)
  USE(cv);
  BAR();
  const u32 tb = __builtin_amdgcn_readfirstlane(*((volatile LAS u32*)(L + L_FL) + F_R));
  u32 thr = tau * (proj ? 38 : 31);
#pragma unroll
  for (int j = 0; j < 32; ++j) {
    PACE();
    v[j] = ld16<AUX_NT>(rv, vo, (j >> 3) * 131072 + (j & 7) * 512); SB();
    if (j == 31) { lds_post(L, F_V); SB(); }
    if (j >= WIN) {
      USE(v[j - WIN]);
      if ((j - WIN) & 1) VPERM((j - WIN) >> 1);
      SB();
    }
  }
  lds_wait(L, F_P, 4 * 64);
  u32 ovo = (lane & 8) ? OOB : (g4 & 1) * 32768 + (lane & 7) * 32 + (lane >> 5) * 16;
  u32 oso = OG_OFF + (kvh * 4 + (w >> 1)) * 16384 + (b >> 1) * 4096 + s * 1024 + (b & 1) * 512 + (w & 1) * 256;
  u32 gvo = lane * 16, gso = OG_OFF + j32 * 16384 + w * 4096, yso = YG_OFF + (b * 28 + j32) * 2048;
  u32 lo = L_OA + (2 * w + (lane >> 5)) * 272 + (lane & 31) * 8;
  PIN(ovo); PINS(oso); PIN(gvo); PINS(gso); PINS(yso); PIN(lo);
  f32x4 acc[8];
  {
    const int m = lane & 15;
    const LAS char* pp = L + L_PS + ((m >> 2) & 1) * 512 + (g4 * 16 + (m & 1) * 8) * 2;
    u32x4 pa[4];
#pragma unroll
    for (int c = 0; c < 4; ++c) pa[c] = *(const LAS u32x4*)(pp + c * 128);
#pragma unroll
    for (int j = 0; j < 8; ++j) acc[j] = f32x4{0.f, 0.f, 0.f, 0.f};
#pragma unroll
    for (int c = 0; c < 4; ++c) {
#pragma unroll
      for (int a = 4 * c; a < 4 * c + 4; ++a) if (2 * a + 1 + WIN > 31 && a < 15) { VPERM(a); SB(); }
      if (c < 3) {
#pragma unroll
        for (int j = 0; j < 8; ++j) acc[j] = mfma(pa[c], fr[c][j], acc[j]);
        SB();
      }
    }
#pragma unroll
    for (int d = 0; d < 4; ++d) {
      const int dd = (d >> 1) + (d & 1) * 2;
      VPERM1(15, dd);
      acc[2 * dd] = mfma(pa[3], fr[3][2 * dd], acc[2 * dd]);
      acc[2 * dd + 1] = mfma(pa[3], fr[3][2 * dd + 1], acc[2 * dd + 1]);
      SB();
    }
  }
  {
    const bool up = lane >= 32;
    float o[4];
#pragma unroll
    for (int j = 0; j < 4; ++j) {
      const float ev_ = up ? acc[j + 4][0] : acc[j][0], od_ = up ? acc[j + 4][1] : acc[j][1];
      o[j] = ev_ + dpp<ROR8>(od_);
      PIN(o[j]);
    }
    st16(rws, ovo, oso, pkbf(o[0], o[1]), pkbf(o[2], o[3]), tag);
  }
  SB();
  u32 tn = now32();
  if (b < 7) {
    lds_wait(L, F_X, 64);
    f32x4 sw = *(const LAS f32x4*)(L + L_WT + (2 * w + (lane >> 5)) * 16);
    PIN(sw);
    until(later(tn + D_A, tb + T_L + X_A));
    u32x4 og[4];
    float c[4];
    gather<4, 0>(rws, gvo, gso, tag, og, [&](int i) {
      c[0] = fmaf(sw[i], bflo(og[i][0]), i ? c[0] : 0.f); c[1] = fmaf(sw[i], bfhi(og[i][0]), i ? c[1] : 0.f);
      c[2] = fmaf(sw[i], bflo(og[i][2]), i ? c[2] : 0.f); c[3] = fmaf(sw[i], bfhi(og[i][2]), i ? c[3] : 0.f);
      PIN(c[0]); PIN(c[1]); PIN(c[2]); PIN(c[3]);
    });
    *(LAS u32x2*)(L + lo) = u32x2{pkbf(c[0], c[1]), pkbf(c[2], c[3])};
    BAR();
    tn = now32();
  }
  if (w != 0 || kvh == 7) return;
  const rsrc_t ro = rsrc(out, 8 * D * 2);
  u32 oo = (lane & 28) ? OOB : (b * D + j32 * 16) * 2 + (lane & 3) * 8 + (lane >> 5) * 4;
  PIN(oo);
  until(later(tn + D_B, tb + T_L + X_B));
  u32x4 y[2];
  float c[4];
  gather<2, 0>(rws, gvo, yso, tag, y, [&](int i) {
    c[0] = bflo(y[i][0]) + (i ? c[0] : 0.f); c[1] = bfhi(y[i][0]) + (i ? c[1] : 0.f);
    c[2] = bflo(y[i][2]) + (i ? c[2] : 0.f); c[3] = bfhi(y[i][2]) + (i ? c[3] : 0.f);
    PIN(c[0]); PIN(c[1]); PIN(c[2]); PIN(c[3]);
  });
  const auto p0 = __builtin_amdgcn_permlane32_swap(asu(c[0]), asu(c[2]), false, false);
  const auto p1 = __builtin_amdgcn_permlane32_swap(asu(c[1]), asu(c[3]), false, false);
  float y0 = xsum16(asf(p0[0]) + asf(p0[1])), y1 = xsum16(asf(p1[0]) + asf(p1[1]));
  y0 += dpp<ROR4>(y0); y1 += dpp<ROR4>(y1);
  y0 += dpp<ROR8>(y0); y1 += dpp<ROR8>(y1);
  __builtin_amdgcn_raw_buffer_store_b32(pkbf(y0, y1), ro, oo, 0, AUX_SC1);
  __builtin_amdgcn_raw_buffer_store_b32(tag, rws, bid == 0 && lane == 0 ? 0 : OOB, EP_OFF, AUX_SC1);
  asm volatile(".Lfd_code_end:");
}

extern "C" void kernel_launch(void* const* d_in, const int* in_sizes, int n_in, void* d_out, int out_size, void* d_ws, size_t ws_size,
                              hipStream_t stream) {
  fused_decode<<<256, 512, 0, stream>>>((const u16*)d_in[0], (const char*)d_in[1], (const char*)d_in[2], (const char*)d_in[3],
                                        (const char*)d_in[4], (const char*)d_in[6], (const char*)d_in[7], (const u16*)d_in[8],
                                        (u16*)d_out, (char*)d_ws);
}
```
